```python
import torch

def _softmax(x, axis=-1):
    return torch.softmax(x, dim=axis)
def _gelu(x):
    return torch.nn.functional.gelu(x, approximate="tanh")
def _silu(x):
    return torch.nn.functional.silu(x)
def _sigmoid(x):
    return torch.sigmoid(x)
def _layernorm(x, axis=-1, eps=1e-5):
    m = x.mean(dim=axis, keepdim=True)
    v = x.var(dim=axis, keepdim=True, unbiased=False)
    return (x - m) / torch.sqrt(v + eps)
def _scatter_add(tgt, idx, src, axis=0):
    return tgt.clone().index_add_(axis, idx, src)
def _segment_sum(src, seg, n):
    out = torch.zeros((n,) + src.shape[1:], dtype=src.dtype)
    return out.index_add_(0, seg, src)
def _pad(x, pw):
    flat = tuple(v for pair in reversed(pw) for v in pair)
    return torch.nn.functional.pad(x, flat)

kernel_name = 'sequential_preLN__softmax_full_mha__dense_activation_relu__prompt'

def setup_inputs():

    _w = (lambda a: (lambda t: t.to(torch.bfloat16) if t.is_floating_point() else t)(torch.as_tensor(a)))
    def _A_setup():
        B, S, D = 1, 2048, 640
        return {
            "x": _w((lambda *s: torch.randn(*s, dtype=torch.bfloat16) * 0.05)(B, S, D)),
            "wqkv": _w((lambda *s: torch.randn(*s, dtype=torch.bfloat16) * 0.05)(D, 3 * D)),
            "wo": _w((lambda *s: torch.randn(*s, dtype=torch.bfloat16) * 0.05)(D, D)),
        }
    _A_in = _A_setup()

    def _B_setup():
        B, S, D, Dff = 1, 2048, 640, 1536
        return {
            "x": _w((lambda *s: torch.randn(*s, dtype=torch.bfloat16) * 0.05)(B, S, D)),
            "w1": _w((lambda *s: torch.randn(*s, dtype=torch.bfloat16) * 0.05)(D, Dff)),
            "w2": _w((lambda *s: torch.randn(*s, dtype=torch.bfloat16) * 0.05)(Dff, D)),
        }
    _B_in = _B_setup()

    return {
        "A_x": _A_in["x"],
        "A_wqkv": _A_in["wqkv"],
        "A_wo": _A_in["wo"],
        "B_x": _B_in["x"],
        "B_w1": _B_in["w1"],
        "B_w2": _B_in["w2"],
    }

def reference(A_x, A_wqkv, A_wo, B_x, B_w1, B_w2):
    def _A_ref(x, wqkv, wo):
        B, S, D = x.shape
        H = 32; Dh = D // H
        qkv = (lambda x, *d: x.permute(*d))((x @ wqkv).reshape(B, S, 3, H, Dh), 2, 0, 3, 1, 4)
        q, k, v = qkv[0], qkv[1], qkv[2]
        scale = Dh ** -0.5
        scores = torch.einsum("bhqd,bhkd->bhqk", q, k) * scale
        p = _softmax(scores, axis=-1)
        o = torch.einsum("bhqk,bhkd->bhqd", p, v)
        return (lambda x, *d: x.permute(*d))(o, 0, 2, 1, 3).reshape(B, S, D) @ wo

    def _B_ref(x, w1, w2):
        h = torch.einsum("bsd,df->bsf", x, w1)
        zero = torch.zeros_like(h)
        h = torch.maximum(h, zero)
        return torch.einsum("bsf,fd->bsd", h, w2)

    x = A_x
    h = x + _A_ref(_layernorm(x, axis=-1), A_wqkv, A_wo)
    h2 = h + _B_ref(_layernorm(h, axis=-1), B_w1, B_w2)
    return h2
```

```cpp
#include <hip/hip_runtime.h>
#include <hip/hip_bf16.h>
typedef unsigned short u16;
static __device__ __forceinline__ float bf2f(u16 v){ return __uint_as_float(((unsigned)v)<<16); }
static __device__ __forceinline__ u16 f2bf(float f){ unsigned u=__float_as_uint(f); u += 0x7fffu + ((u>>16)&1u); return (u16)(u>>16); }
constexpr int S=2048, D=640, H=32, DH=20, FF=1536;

__global__ void ln_k(const u16* x, u16* y){
  int row=blockIdx.x; int l=threadIdx.x;
  float v[10]; float s=0;
  for(int i=0;i<10;i++){ v[i]=bf2f(x[row*D+l+64*i]); s+=v[i]; }
  for(int o=32;o;o>>=1) s+=__shfl_xor(s,o);
  float m=s/D; float q=0;
  for(int i=0;i<10;i++){ float d=v[i]-m; q+=d*d; }
  for(int o=32;o;o>>=1) q+=__shfl_xor(q,o);
  float r=rsqrtf(q/D+1e-5f);
  for(int i=0;i<10;i++) y[row*D+l+64*i]=f2bf((v[i]-m)*r);
}
template<int RELU,int RES>
__global__ void gemm_k(const u16* A,const u16* B,u16* C,const u16* R,int M,int N,int K){
  __shared__ float As[16][65]; __shared__ float Bs[16][65];
  int tx=threadIdx.x&15, ty=threadIdx.x>>4;
  int bm=blockIdx.y*64, bn=blockIdx.x*64;
  float acc[4][4]={};
  for(int k0=0;k0<K;k0+=16){
    for(int i=threadIdx.x;i<64*16;i+=256){ int r=i>>4,c=i&15; As[c][r]=bf2f(A[(bm+r)*K+k0+c]); }
    for(int i=threadIdx.x;i<64*16;i+=256){ int r=i>>6,c=i&63; Bs[r][c]=bf2f(B[(k0+r)*N+bn+c]); }
    __syncthreads();
    for(int k=0;k<16;k++){
      float a[4],b[4];
      for(int i=0;i<4;i++){a[i]=As[k][ty*4+i]; b[i]=Bs[k][tx*4+i];}
      for(int i=0;i<4;i++)for(int j=0;j<4;j++)acc[i][j]+=a[i]*b[j];
    }
    __syncthreads();
  }
  for(int i=0;i<4;i++)for(int j=0;j<4;j++){
    int r=bm+ty*4+i,c=bn+tx*4+j; float v=acc[i][j];
    if(RELU) v=fmaxf(v,0.f);
    if(RES) v=bf2f(f2bf(v))+bf2f(R[r*N+c]);
    C[r*N+c]=f2bf(v);
  }
}
__global__ void attn_k(const u16* qkv,u16* o){
  int h=blockIdx.y; int q=blockIdx.x*256+threadIdx.x;
  __shared__ float Ks[64][20], Vs[64][20];
  float qv[20]; for(int d=0;d<20;d++) qv[d]=bf2f(qkv[q*1920+h*20+d])*0.22360679775f;
  float m=-1e30f,l=0,acc[20]={};
  for(int k0=0;k0<S;k0+=64){
    __syncthreads();
    for(int i=threadIdx.x;i<64*20;i+=256){int r=i/20,c=i%20; Ks[r][c]=bf2f(qkv[(k0+r)*1920+640+h*20+c]); Vs[r][c]=bf2f(qkv[(k0+r)*1920+1280+h*20+c]);}
    __syncthreads();
    for(int j=0;j<64;j++){
      float s=0; for(int d=0;d<20;d++) s+=qv[d]*Ks[j][d];
      float mn=fmaxf(m,s); float a=__expf(m-mn), p=__expf(s-mn);
      l=l*a+p; for(int d=0;d<20;d++) acc[d]=acc[d]*a+p*Vs[j][d];
      m=mn;
    }
  }
  for(int d=0;d<20;d++) o[q*640+h*20+d]=f2bf(acc[d]/l);
}
extern "C" void kernel_launch(void* const* d_in, const int* in_sizes, int n_in, void* d_out, int out_size, void* d_ws, size_t ws_size, hipStream_t stream){
  const u16* x=(const u16*)d_in[0]; const u16* wqkv=(const u16*)d_in[1]; const u16* wo=(const u16*)d_in[2];
  const u16* w1=(const u16*)d_in[4]; const u16* w2=(const u16*)d_in[5];
  u16* ws=(u16*)d_ws; u16* ln=ws; u16* qkv=ln+S*D; u16* o=qkv+S*1920; u16* h=o+S*D; u16* h1=h+S*D;
  ln_k<<<S,64,0,stream>>>(x,ln);
  gemm_k<0,0><<<dim3(1920/64,S/64),256,0,stream>>>(ln,wqkv,qkv,nullptr,S,1920,D);
  attn_k<<<dim3(S/256,H),256,0,stream>>>(qkv,o);
  gemm_k<0,1><<<dim3(D/64,S/64),256,0,stream>>>(o,wo,h,x,S,D,D);
  ln_k<<<S,64,0,stream>>>(h,ln);
  gemm_k<1,0><<<dim3(FF/64,S/64),256,0,stream>>>(ln,w1,h1,nullptr,S,FF,D);
  gemm_k<0,1><<<dim3(D/64,S/64),256,0,stream>>>(h1,w2,(u16*)d_out,h,S,D,FF);
}
```

```cpp
#include <hip/hip_runtime.h>
#include <stdint.h>

typedef unsigned short u16;
typedef unsigned u32x4 __attribute__((ext_vector_type(4)));
typedef unsigned u32x2 __attribute__((ext_vector_type(2)));
typedef float f32x2 __attribute__((ext_vector_type(2)));
typedef float f32x4 __attribute__((ext_vector_type(4)));
typedef float f32x8 __attribute__((ext_vector_type(8)));
typedef float f32x16 __attribute__((ext_vector_type(16)));
typedef float f32x32 __attribute__((ext_vector_type(32)));
typedef __bf16 bf16x2 __attribute__((ext_vector_type(2)));
typedef __bf16 bf16x8 __attribute__((ext_vector_type(8)));
typedef __bf16 bf16x16 __attribute__((ext_vector_type(16)));
typedef short s16x4 __attribute__((ext_vector_type(4)));
#define LAS __attribute__((address_space(3)))
#define DI __device__ __forceinline__

#define AB 0
#define DA0 5
#define DA1 4
#define DA2 5
#define ASC 16
#define KS 0
#define SLP 24
#define QAUX 16
#define SL0 0
#define SLQ 1
#define QLA 3
#define LF 1
#define XS 24
#define YL 0
#define SL2 0
#define KB2 (16 - DA2)
#define MAP 0
#define F1 0
#define SL1 0
#define SLH 8
#define W8 36
#define ND 20
#define W1W 1
#define QU 1
#define QW 0
#define XG 72
#define CLIM 40064
#define PK 7
#define KB1 5
#define SL4 8
#define D11 20
constexpr int S = 2048, D = 640, FF = 1536;
constexpr float LN_EPS = 1e-5f;
constexpr float QSCALE = 0.22360679774997896f * 1.4426950408889634f;

constexpr size_t WS_Q = 0, WS_KV = WS_Q + 2621440, WS_O = WS_KV + 5242880, WS_H = WS_O + 2621440, WS_ST = WS_H + 2621440,
                 WS_H1 = WS_ST + 131072, WS_WO = WS_H1 + 6291456, WS_W1 = WS_WO + 819200, WS_W2 = WS_W1 + 1966080, WS_CS = WS_W2 + 1966080,
                 WS_FL = WS_CS + 8192, WS_EP = WS_FL + 393216, WS_XS = WS_EP + 128, WS_END = WS_XS + 32768;

DI unsigned cvtpk(float lo, float hi) { f32x2 v = {lo, hi}; return __builtin_bit_cast(unsigned, __builtin_convertvector(v, bf16x2)); }
DI float bflo(unsigned u) { return __uint_as_float(u << 16); }
DI float bfhi(unsigned u) { return __uint_as_float(u & 0xffff0000u); }
#define GL "global_load_lds_dwordx4 %1, %2"
#define DMA_ASM(SC) \
  if constexpr (N == 1) asm volatile("s_mov_b32 m0, %0\n\ts_nop 0\n\t" GL SC ::"s"(ldsdst), "v"(voff), "s"(base) : "memory"); \
  if constexpr (N == 2) asm volatile("s_mov_b32 m0, %0\n\ts_nop 0\n\t" GL SC "\n\t" GL " offset:1024" SC ::"s"(ldsdst), "v"(voff), "s"(base) : "memory"); \
  if constexpr (N == 3) asm volatile("s_mov_b32 m0, %0\n\ts_nop 0\n\t" GL SC "\n\t" GL " offset:1024" SC "\n\t" GL " offset:2048" SC ::"s"(ldsdst), "v"(voff), "s"(base) : "memory"); \
  if constexpr (N == 4) asm volatile("s_mov_b32 m0, %0\n\ts_nop 0\n\t" GL SC "\n\t" GL " offset:1024" SC "\n\t" GL " offset:2048" SC "\n\t" GL " offset:3072" SC ::"s"(ldsdst), "v"(voff), "s"(base) : "memory");
template <int N> DI void dma(unsigned ldsdst, unsigned voff, const void* base) { DMA_ASM("") }
template <int N> DI void dmac(unsigned ldsdst, unsigned voff, const void* base) { DMA_ASM(" sc1") }
template <int N> DI void waitv() { asm volatile("s_waitcnt vmcnt(%0)" ::"n"(N) : "memory"); }
template <int N> DI void waitv_bar() { asm volatile("s_waitcnt vmcnt(%0)\n\ts_barrier" ::"n"(N) : "memory"); }
DI void bar() { asm volatile("s_barrier" ::: "memory"); }
DI void waitl_bar() { asm volatile("s_waitcnt lgkmcnt(0)\n\ts_barrier" ::: "memory"); }
DI bf16x8 ldsr128(unsigned a) { return *(const LAS bf16x8*)(size_t)a; }
DI s16x4 ldstr(unsigned a) { return __builtin_amdgcn_ds_read_tr16_b64_v4i16((LAS s16x4*)(size_t)a); }
#define SB() __builtin_amdgcn_sched_barrier(0)
#define SGB(m, n) __builtin_amdgcn_sched_group_barrier(m, n, 0)
DI float sum4(float v) {
  auto a = __builtin_amdgcn_permlane16_swap(__float_as_uint(v), __float_as_uint(v), false, false); v = __uint_as_float(a[0]) + __uint_as_float(a[1]);
  auto c = __builtin_amdgcn_permlane32_swap(__float_as_uint(v), __float_as_uint(v), false, false); return __uint_as_float(c[0]) + __uint_as_float(c[1]);
}
template <int C> DI float dpp_add(float v) { return v + __uint_as_float(__builtin_amdgcn_update_dpp(0u, __float_as_uint(v), C, 0xf, 0xf, true)); }
DI float sum32(float v) {
  v = dpp_add<0x140>(dpp_add<0x141>(dpp_add<0x4e>(dpp_add<0xb1>(v))));
  auto a = __builtin_amdgcn_permlane16_swap(__float_as_uint(v), __float_as_uint(v), false, false); return __uint_as_float(a[0]) + __uint_as_float(a[1]);
}
DI unsigned opq(unsigned a) { asm("" : "+v"(a)); return a; }
template <int NM, int NR, int I = 0> DI void ilv() {
  if constexpr (I < NM) { SGB(0x8, 1); constexpr int n = (NR * (I + 1) + NM - 1) / NM - (NR * I + NM - 1) / NM; if constexpr (n > 0) SGB(0x100, n); ilv<NM, NR, I + 1>(); }
}
template <int I = 0> DI void ilvq() {
  if constexpr (I < 10) { SGB(0x8, 1); if constexpr (I < 6) SGB(0x100, 2); else SGB(0x2, 2); ilvq<I + 1>(); }
}
#define MFMA32(a, b, c) __builtin_amdgcn_mfma_f32_32x32x16_bf16(a, b, c, 0, 0, 0)
#define MFMA16(a, b, c) __builtin_amdgcn_mfma_f32_16x16x32_bf16(a, b, c, 0, 0, 0)
DI const char* mega_code() {
  unsigned lo, hi;
  asm("s_getpc_b64 vcc\n\ts_add_u32 %0, vcc_lo, _Z6mega_kPKcS0_S0_S0_S0_PcS1_@rel32@lo+4\n\ts_addc_u32 %1, vcc_hi, _Z6mega_kPKcS0_S0_S0_S0_PcS1_@rel32@hi+12" : "=s"(lo), "=s"(hi)::"vcc", "scc");
  return (const char*)(((size_t)hi << 32) | lo);
}
typedef __amdgpu_buffer_rsrc_t rsrc_t;
DI rsrc_t mk_rsrc(const void* p, unsigned bytes) { return __builtin_amdgcn_make_buffer_rsrc((void*)p, 0, bytes, 0x00020000); }
template <int PITCH> DI u32x4 gather8(rsrc_t r, unsigned so, unsigned lo, unsigned hi, unsigned two = 2 * PITCH) {
  u32x4 o;
  unsigned v[8];
#pragma unroll
  for (int j = 0; j < 8; ++j) v[j] = __builtin_amdgcn_raw_buffer_load_b16(r, j < 4 ? lo : hi, so + (j & 1) * PITCH + ((j & 2) ? two : 0), 0);
#pragma unroll
  for (int j = 0; j < 4; ++j) o[j] = v[2 * j] | (v[2 * j + 1] << 16);
  return o;
}

constexpr int SC1 = 16;
typedef unsigned long long u64;
typedef __attribute__((address_space(1))) u64 gu64;
DI void st16(u32x4 v, rsrc_t r, unsigned off) { __builtin_amdgcn_raw_buffer_store_b128(v, r, off, 0, SC1); }
DI void st8(u32x2 v, rsrc_t r, unsigned off) { __builtin_amdgcn_raw_buffer_store_b64(v, r, off, 0, SC1); }
DI void publish(char* ws, u64 tag, int word, int lane) {
  asm volatile("s_waitcnt vmcnt(0)" ::: "memory");
  if (lane == 0) __hip_atomic_store((gu64*)(ws + WS_FL) + word, tag, __ATOMIC_RELAXED, __HIP_MEMORY_SCOPE_AGENT);
}
template <int SL = 1> DI void await(char* ws, u64 tag, int word, int word2 = -1) {
  gu64* p = (gu64*)(ws + WS_FL);
#pragma unroll 1
  for (unsigned spin = 0; spin < (1u << 20); ++spin) {
    bool ok = __hip_atomic_load(p + word, __ATOMIC_RELAXED, __HIP_MEMORY_SCOPE_AGENT) == tag;
    if (!__builtin_constant_p(word2)) ok &= __hip_atomic_load(p + word2, __ATOMIC_RELAXED, __HIP_MEMORY_SCOPE_AGENT) == tag;
    if (__all(ok)) break;
    __builtin_amdgcn_s_sleep(SL);
  }
  asm volatile("" ::: "memory");
}
template <int KB> DI void await_panel(char* ws, u64 tag, int word, const char* pf, int lane) {
  u64 v; unsigned t, n;
  asm volatile("s_mov_b32 %2, 0x100000\n"
               "1:\n\tglobal_load_dwordx2 %0, %3, %6 sc1\n\tglobal_load_dword %1, %4, %7 sc1\n\tglobal_load_dword %1, %5, %7 sc1\n\ts_waitcnt vmcnt(2)\n\t"
               "v_cmp_ne_u64 vcc, %8, %0\n\ts_cbranch_vccz 2f\n\ts_sleep 1\n\ts_sub_u32 %2, %2, 1\n\ts_cbranch_scc0 1b\n"
               "2:\n\ts_barrier\n\ts_waitcnt vmcnt(0)"
               : "=&v"(v), "=&v"(t), "=&s"(n) : "v"(word * 8), "v"(lane * 128), "v"(8192 + (lane & 15) * 128), "s"(ws + WS_FL), "s"(pf), "s"(tag) : "vcc", "scc", "memory");
}
DI u64 get_tag(const char* ws) {
  u64 e;
  asm volatile("s_load_dwordx2 %0, %1, 0x0\n\ts_waitcnt lgkmcnt(0)" : "=s"(e) : "s"(ws + WS_EP) : "memory");
  return e * 6364136223846793005ull + 1442695040888963407ull;
}
DI int qkv_wg(int rt, int ct) { return (rt >> 2) + 4 * (ct >> 3) + 8 * ((rt & 3) + 4 * (ct & 7)); }

template <int NB> DI unsigned x_stats(const char* x, char* ws, int row0, int lane) {
  const int row = row0 + (lane >> 5), c = lane & 31;
  const char* p = x + (size_t)row * 1280;
  const u32x4 a = *(const u32x4*)(p + c * 16), b = *(const u32x4*)(p + 512 + c * 16); const u32x2 d = *(const u32x2*)(p + 1024 + c * 8);
  const unsigned tag = (unsigned)get_tag(ws);
#pragma unroll
  for (int i = 0; i < NB; ++i) bar();
  const unsigned v[10] = {a[0], a[1], a[2], a[3], b[0], b[1], b[2], b[3], d[0], d[1]};
  float s1 = 0.f, s2 = 0.f;
#pragma unroll
  for (int i = 0; i < 10; ++i) { const float lo = bflo(v[i]), hi = bfhi(v[i]); s1 += lo + hi; s2 = __builtin_fmaf(lo, lo, __builtin_fmaf(hi, hi, s2)); }
  s1 = sum32(s1); s2 = sum32(s2);
  const float mean = s1 * (1.f / D), var = fmaxf(s2 * (1.f / D) - mean * mean, 0.f);
  if (c < 2) st8((u32x2){__float_as_uint(c ? -mean : __builtin_amdgcn_rsqf(var + LN_EPS)), tag}, mk_rsrc(ws + WS_XS, 32768), row * 16 + c * 8);
  return tag;
}
DI bf16x8 mk_sel(int lane) { unsigned o = ((lane & 15) == ((lane >> 4) & 1)) ? 0x3F803F80u : 0u; u32x4 t = {o, o, o, o}; return __builtin_bit_cast(bf16x8, t); }
struct QF { bf16x8 xa[2][2], wb[2][2]; };
template <bool ISV, class F> DI void qkv_loop(unsigned L0, int lane, int wm, int wn, f32x16 (&acc)[2][2], F tables) {
  f32x4 cs = {0.f, 0.f, 0.f, 0.f};
  constexpr int NS = 10, R = 5, SLOT = 32768;
  const int r31 = lane & 31, hh = lane >> 5;
  unsigned aoff[4], boff[4];
#pragma unroll
  for (int s = 0; s < 4; ++s) { int row = 64 * wm + r31; aoff[s] = L0 + row * 128 + (((2 * s + hh) ^ ((row >> 1) & 7)) << 4); }
#pragma unroll
  for (int c = 0; c < 2; ++c)
#pragma unroll
    for (int t = 0; t < 2; ++t) { int blk = (lane >> 4) & 1, q = (lane & 15) >> 2, p = lane & 3, row = 8 * hh + 4 * t + q, ch = 4 * (2 * wn + (c ^ wm)) + 2 * blk + (p >> 1);
      boff[2 * c + t] = L0 + 16384 + 256 * row + ((ch ^ (((row & 3) << 2) | ((row >> 2) & 3))) << 4) + 8 * (p & 1); }
  const bf16x8 sel = mk_sel(lane);
  unsigned pa[4], pb[4];
  auto setp = [&](unsigned so) {
#pragma unroll
    for (int i = 0; i < 4; ++i) { pa[i] = opq(so + aoff[i]); pb[i] = opq(so + boff[i]); } };
  auto rd = [&](QF& f, int half) {
#pragma unroll
    for (int k = 0; k < 2; ++k) { const int ks = 2 * half + k;
      f.xa[k][0] = ldsr128(pa[ks]); f.xa[k][1] = ldsr128(pa[ks] + 4096);
#pragma unroll
      for (int c = 0; c < 2; ++c) { s16x4 lo = ldstr(pb[2 * c] + ks * 4096), hi = ldstr(pb[2 * c + 1] + ks * 4096);
        f.wb[k][c] = __builtin_bit_cast(bf16x8, __builtin_shufflevector(lo, hi, 0, 1, 2, 3, 4, 5, 6, 7)); } }
  };
  auto mm = [&](const QF& f) {
#pragma unroll
    for (int k = 0; k < 2; ++k) {
#pragma unroll
      for (int i = 0; i < 2; ++i)
#pragma unroll
        for (int c = 0; c < 2; ++c) acc[i][c] = ISV ? MFMA32(f.xa[k][i], f.wb[k][c], acc[i][c]) : MFMA32(f.wb[k][c], f.xa[k][i], acc[i][c]);
      if (AB != 3) cs = MFMA16(sel, f.wb[k][0], cs); }
  };
  QF f0, f1;
  setp(0); bar();
  rd(f0, 0); SB(); rd(f1, 1); mm(f0); ilv<10, 12>(); SB();
  unsigned so = SLOT;
  setp(so);
#pragma unroll QU
  for (int s = 1; s < NS; ++s) {
    bar();
    if (QW) { __builtin_amdgcn_s_waitcnt(0xC07F); SB(); }
    rd(f0, 0); mm(f1); ilv<10, 12>(); SB();
    if (QW) { __builtin_amdgcn_s_waitcnt(0xC07F); SB(); }
    rd(f1, 1); so = (so == (R - 1) * SLOT) ? 0 : so + SLOT;
    mm(f0); setp(so); ilvq(); SB();
  }
  cs = MFMA16(sel, f1.wb[0][0], cs); cs = MFMA16(sel, f1.wb[1][0], cs); SB();
#pragma unroll
  for (int k = 0; k < 2; ++k) {
#pragma unroll
    for (int i = 0; i < 2; ++i)
#pragma unroll
      for (int c = 0; c < 2; ++c) acc[i][c] = ISV ? MFMA32(f1.xa[k][i], f1.wb[k][c], acc[i][c]) : MFMA32(f1.wb[k][c], f1.xa[k][i], acc[i][c]);
    SB();
    if (k == 0) {
      if (lane < 16) { LAS float* t = (LAS float*)(size_t)(L0 + (256 + 64 * wn + 32 * wm + lane) * 4); t[0] = cs[0]; t[16] = cs[1]; }
      waitl_bar();
      tables(); SB();
    }
  }
}
DI void qkv_side(int sp, int wid, int lane, const char* wo, const char* w1, char* ws, char* lds) {
  if (wid < 10) {
    const int rg = lane / 12, ch = lane - rg * 12;
    u32x4 v[16];
#pragma unroll
    for (int t = 0; t < 16; ++t) v[t] = __builtin_amdgcn_raw_buffer_load_b128(mk_rsrc(w1, 1966080), rg < 4 ? (64 * wid + rg) * 3072 + sp * 192 + ch * 16 : 0x7fffffff, t * 4 * 3072, 0);
    const int grp = sp * 10 + wid, ct = grp / 20, ks = grp - ct * 20, i = lane & 15, c = 4 * ks + (lane >> 4), g = c / 5, r = c - g * 5;
    const unsigned col = (80 * ct + 20 * (i >> 2) + (i & 3)) * 2;
    const unsigned lo = (r < 4 ? (2 * g + (r >> 1)) * 20 + 4 * (r & 1) : 40 * g + 16) * 1280 + col, hi = lo + (r < 4 ? 8 : 20) * 1280;
    u32x4 f[5];
#pragma unroll
    for (int cb = 0; cb < 5; ++cb) f[cb] = gather8<1280>(mk_rsrc(wo, 819200), cb * 8, lo, hi);
    SB();
    float s[8] = {0.f, 0.f, 0.f, 0.f, 0.f, 0.f, 0.f, 0.f};
#pragma unroll
    for (int t = 0; t < 16; ++t)
#pragma unroll
      for (int e = 0; e < 4; ++e) { s[2 * e] += bflo(v[t][e]); s[2 * e + 1] += bfhi(v[t][e]); }
    if (rg < 4) {
      f32x4* q = (f32x4*)(lds + ((wid * 4 + rg) * 96 + ch * 8) * 4);
      q[0] = (f32x4){s[0], s[1], s[2], s[3]}; q[1] = (f32x4){s[4], s[5], s[6], s[7]};
    }
#pragma unroll
    for (int cb = 0; cb < 5; ++cb) st16(f[cb], mk_rsrc(ws + WS_WO, 819200), (grp * 5 + cb) * 1024 + lane * 16);
  }
  waitl_bar();
  const int t = wid * 64 + lane;
  if (t < 96) {
    float c = 0.f;
#pragma unroll
    for (int k = 0; k < 40; ++k) c += ((const float*)lds)[k * 96 + t];
    __builtin_amdgcn_raw_buffer_store_b32(__float_as_uint(c), mk_rsrc(ws + WS_CS, 6144), (sp * 96 + t) * 4, 0, SC1);
  }
}
DI void qkv_phase(const char* __restrict__ x, const char* __restrict__ w, const char* __restrict__ wo, const char* __restrict__ w1, char* __restrict__ ws, char* lds, int lane, int wid) {
  const int b = blockIdx.x, xc = b & 7, j = b >> 3;
  const int rt = (xc & 3) * 4 + (j & 3), ct = (xc >> 2) * 8 + (j >> 2);
  if (__builtin_expect(ct >= 15, 0)) {
    if (wid == 10) { unsigned o = min(lane, 11) * 128; asm volatile("global_load_dword %0, %0, %1 sc1\n\ts_waitcnt vmcnt(0)" : "+v"(o) : "s"(ws + WS_FL + b * 1536) : "memory"); }
    qkv_side(rt, wid, lane, wo, w1, ws, lds); return; }
  const unsigned L0 = (unsigned)(size_t)lds;
  constexpr int NS = 10, R = 5, LA = QLA, SLOT = 32768;
  float* stf = (float*)lds;
  if (__builtin_expect(wid >= 8, LF)) {
    const int hf = (wid - 8) >> 1;
    const bool isw = wid & 1;
    const int row = lane >> 3, k = lane >> 4;
    const unsigned v0 = isw ? k * 3840 + (((lane & 15) ^ (k << 2)) << 4) : row * 1280 + (((lane & 7) ^ (row >> 1)) << 4);
    unsigned vb[4];
#pragma unroll
    for (int i = 0; i < 4; ++i) vb[i] = isw ? (v0 ^ (i << 4)) + i * 4 * 3840 : (v0 ^ ((i & 1) << 6)) + i * 8 * 1280;
    const char* p = isw ? w + ct * 256 + hf * 32 * 3840 : x + (size_t)(rt * 128 + hf * 64) * 1280;
    const unsigned hop = isw ? 16 * 3840 : 32 * 1280, step = isw ? 64 * 3840 : 128, d0 = L0 + hf * 8192 + (isw ? 16384 : 0);
    unsigned dst = d0;
#pragma unroll 1
    for (int s = -LA; s < NS - LA; ++s) {
      if (s >= 0) { waitv_bar<8 * LA - 8>(); }
#pragma unroll
      for (int i = 0; i < 8; ++i) dma<1>(dst + i * 1024, vb[i & 3], p + (i >> 2) * hop);
      p += step; dst = dst == d0 + (R - 1) * SLOT ? d0 : dst + SLOT;
    }
    if (LA == 4) waitv_bar<24>();
    waitv_bar<16>(); waitv_bar<8>(); waitv_bar<0>();
    if (wid == 11 && j < 5) {
      unsigned o = min((j + 1) * 8192 + lane * 128, CLIM);
      asm volatile("global_load_dword %0, %0, %1\n\ts_barrier\n\ts_waitcnt vmcnt(0)" : "+v"(o) : "s"(mega_code()) : "memory");
    } else bar();
    return;
  }
  if (wid >= 4) {
    unsigned tag;
#pragma unroll 1
    for (int r = 0; r < 2; ++r) { if (r == 0 || wid - 4 == ct) tag = x_stats<1>(x, ws, rt * 128 + (r ? 15 : ct) * 8 + (wid - 4) * 2, lane); else bar(); }
    if (wid == 6) {
      unsigned o = min(lane, 11) * 128;
      asm volatile("global_load_dword %0, %0, %1 sc1\n\ts_barrier\n\ts_barrier\n\ts_barrier\n\ts_barrier\n\ts_waitcnt vmcnt(0)" : "+v"(o) : "s"(ws + WS_FL + b * 1536) : "memory");
    } else { bar(); bar(); bar(); bar(); }
    gu64* p = (gu64*)(ws + WS_XS) + (rt * 128 + (wid - 4) * 32) * 2 + lane;
    u64 v = __hip_atomic_load(p, __ATOMIC_RELAXED, __HIP_MEMORY_SCOPE_AGENT);
    bar(); bar(); bar(); bar();
#pragma unroll 1
    for (unsigned spin = 0; spin < (1u << 20) && !__all((unsigned)(v >> 32) == tag); ++spin) v = __hip_atomic_load(p, __ATOMIC_RELAXED, __HIP_MEMORY_SCOPE_AGENT);
    stf[(lane & 1) * 128 + (wid - 4) * 32 + (lane >> 1)] = __uint_as_float((unsigned)v) * (!(lane & 1) && ct < 5 ? QSCALE : 1.f);
    waitl_bar();
    return;
  }
  const int wm = wid >> 1, wn = wid & 1, r31 = lane & 31, hh = lane >> 5;
  f32x16 acc[2][2] = {};
  const int T0 = rt * 4 + wm * 2;
  if (ct >= 10) {
    unsigned off[2];
#pragma unroll
    for (int c = 0; c < 2; ++c) { const int n = (ct - 10) * 128 + 64 * wn + 32 * (c ^ wm) + r31, head = n / 20, d = n - head * 20; off[c] = opq(head * 163840 + d * 32 + (hh ^ (d >> 4)) * 16); }
    const float* sp = stf + 64 * wm + 4 * hh;
    float csn[2];
    f32x4 ta[2][4];
    qkv_loop<true>(L0, lane, wm, wn, acc, [&]() {
#pragma unroll
      for (int c = 0; c < 2; ++c) csn[c] = stf[256 + 64 * wn + 32 * (c ^ wm) + r31];
#pragma unroll
      for (int s = 0; s < 2; ++s) { ta[s][0] = *(const f32x4*)(sp + 16 * s); ta[s][1] = *(const f32x4*)(sp + 16 * s + 8); ta[s][2] = *(const f32x4*)(sp + 128 + 16 * s); ta[s][3] = *(const f32x4*)(sp + 128 + 16 * s + 8); }
    });
    char* kv = ws + WS_KV + (T0 >> 1) * 5120 + 2560;
#pragma unroll
    for (int i = 0; i < 2; ++i)
#pragma unroll
      for (int s = 0; s < 2; ++s) {
        const f32x4 a0 = i ? *(const f32x4*)(sp + 32 + 16 * s) : ta[s][0], a1 = i ? *(const f32x4*)(sp + 32 + 16 * s + 8) : ta[s][1], m0 = i ? *(const f32x4*)(sp + 160 + 16 * s) : ta[s][2], m1 = i ? *(const f32x4*)(sp + 160 + 16 * s + 8) : ta[s][3];
#pragma unroll
        for (int c = 0; c < 2; ++c) {
          float v[8];
#pragma unroll
          for (int e = 0; e < 4; ++e) { v[e] = a0[e] * __builtin_fmaf(m0[e], csn[c], acc[i][c][8 * s + e]); v[4 + e] = a1[e] * __builtin_fmaf(m1[e], csn[c], acc[i][c][8 * s + 4 + e]); }
          __builtin_amdgcn_raw_buffer_store_b128((u32x4){cvtpk(v[0], v[1]), cvtpk(v[2], v[3]), cvtpk(v[4], v[5]), cvtpk(v[6], v[7])}, mk_rsrc(kv, 0x7fffffff), off[c] + (i * 1280 + s * 640), 0, QAUX);
        }
      }
  } else {
    const bool isQ = ct < 5;
    unsigned om[2][2], orr[2][2];
    const int hstride = isQ ? 81920 : 163840, pb = (isQ ? ct : ct - 5) * 16 + 8 * wn;
    {
      const int slot = isQ ? r31 : ((r31 & 16) | ((r31 & 4) << 1) | ((r31 & 8) >> 1) | (r31 & 3));
#pragma unroll
      for (int c = 0; c < 2; ++c)
#pragma unroll
        for (int gp = 0; gp < 2; ++gp) {
          const int P = pb + 4 * (c ^ wm) + 2 * gp + hh, hp = (P * 205) >> 10, p5 = P - hp * 5;
          om[c][gp] = opq(p5 == 2 ? 0x80000000u : (2 * hp + (p5 > 2)) * hstride + (p5 > 2 ? p5 - 3 : p5) * 512 + slot * 16);
          orr[c][gp] = opq(p5 == 2 ? 2 * hp * hstride + 2048 + slot * 16 : 0x80000000u);
        }
    }
    float a[2], mb[2];
    f32x4 tc[2][4];
    qkv_loop<false>(L0, lane, wm, wn, acc, [&]() {
#pragma unroll
      for (int i = 0; i < 2; ++i) { a[i] = stf[64 * wm + 32 * i + r31]; mb[i] = stf[128 + 64 * wm + 32 * i + r31]; }
#pragma unroll
      for (int c = 0; c < 2; ++c)
#pragma unroll
        for (int g = 0; g < 4; ++g) tc[c][g] = *(const f32x4*)&stf[256 + 64 * wn + 32 * (c ^ wm) + 8 * g + 4 * hh];
    });
    char* tb = isQ ? ws + WS_Q + (size_t)T0 * 1280 : ws + WS_KV + (T0 >> 1) * 5120;
    const rsrc_t rt_ = mk_rsrc(tb, 0x7fffffff);
#pragma unroll
    for (int c = 0; c < 2; ++c)
#pragma unroll
      for (int gp = 0; gp < 2; ++gp) {
        u32x4 o[2];
#pragma unroll
        for (int i = 0; i < 2; ++i) {
          unsigned t[2][2];
#pragma unroll
          for (int q = 0; q < 2; ++q) {
            const f32x4 cv = tc[c][2 * gp + q];
            float v[4];
#pragma unroll
            for (int e = 0; e < 4; ++e) v[e] = a[i] * __builtin_fmaf(mb[i], cv[e], acc[i][c][8 * gp + 4 * q + e]);
            t[q][0] = cvtpk(v[0], v[1]); t[q][1] = cvtpk(v[2], v[3]);
          }
          const auto s0 = __builtin_amdgcn_permlane32_swap(t[0][0], t[1][0], false, false), s1 = __builtin_amdgcn_permlane32_swap(t[0][1], t[1][1], false, false);
          o[i] = (u32x4){s0[0], s1[0], s0[1], s1[1]};
          __builtin_amdgcn_raw_buffer_store_b128(o[i], rt_, om[c][gp] + i * 1024, 0, QAUX);
        }
        const int p0 = (pb + 4 * (c ^ wm) + 2 * gp) % 5;
        if (p0 == 1 || p0 == 2) {
          const bool sw = !isQ && (r31 & 16);
          const u32x4 f = sw ? o[1] : o[0], g = sw ? o[0] : o[1];
          __builtin_amdgcn_raw_buffer_store_b128((u32x4){f[0], f[1], g[0], g[1]}, rt_, orr[c][gp], 0, QAUX);
          __builtin_amdgcn_raw_buffer_store_b128((u32x4){f[2], f[3], g[2], g[3]}, rt_, orr[c][gp] + hstride, 0, QAUX);
        }
      }
  }
}

#define ATTN_ASM \
  "global_load_dwordx4 v[96:99], %[qoff], %[qp] sc1\n\t" \
  "global_load_dwordx2 v[100:101], %[qroff], %[qp] sc1\n\t" \
  "v_mov_b32 v144, 0xdddd\n\t" \
  "v_mov_b64 v[112:113], %[ones]\n\t" \
  "v_mov_b64 v[114:115], %[ones]\n\t" \
  "v_mov_b64 v[116:117], %[ones]\n\t" \
  "v_mov_b64 v[118:119], %[ones]\n\t" \
  "v_mov_b64 v[120:121], %[ones]\n\t" \
  "v_mov_b64 v[122:123], %[ones]\n\t" \
  "v_mov_b64 v[124:125], %[ones]\n\t" \
  "v_mov_b64 v[126:127], %[ones]\n\t" \
  "v_mov_b64 v[128:129], %[ones]\n\t" \
  "v_mov_b64 v[130:131], %[ones]\n\t" \
  "v_mov_b64 v[132:133], %[ones]\n\t" \
  "v_mov_b64 v[134:135], %[ones]\n\t" \
  "v_mov_b64 v[136:137], %[ones]\n\t" \
  "v_mov_b64 v[138:139], %[ones]\n\t" \
  "v_mov_b64 v[140:141], %[ones]\n\t" \
  "v_mov_b64 v[142:143], %[ones]\n\t" \
  "v_mov_b64 v[64:65], 0\n\t" \
  "v_mov_b64 v[66:67], 0\n\t" \
  "v_mov_b64 v[68:69], 0\n\t" \
  "v_mov_b64 v[70:71], 0\n\t" \
  "v_mov_b64 v[72:73], 0\n\t" \
  "v_mov_b64 v[74:75], 0\n\t" \
  "v_mov_b64 v[76:77], 0\n\t" \
  "v_mov_b64 v[78:79], 0\n\t" \
  "v_mov_b64 v[56:57], 0\n\t" \
  "v_mov_b64 v[58:59], 0\n\t" \
  "v_mov_b64 v[60:61], 0\n\t" \
  "v_mov_b64 v[62:63], 0\n\t" \
  "s_waitcnt vmcnt(0)\n\t" \
  "s_barrier\n\t" \
  "ds_read_b128 v[102:105], v145 offset:0\n\t" \
  "ds_read_b128 v[106:109], v145 offset:1024\n\t" \
  "ds_read_b64 v[110:111], v146 offset:0\n\t" \
  "s_mov_b64 exec, %[vmask]\n\t" \
  "ds_read_b128 v[112:115], v147 offset:0\n\t" \
  "ds_read_b128 v[116:119], v147 offset:640\n\t" \
  "ds_read_b128 v[120:123], v147 offset:1280\n\t" \
  "ds_read_b128 v[124:127], v147 offset:1920\n\t" \
  "s_mov_b64 exec, -1\n\t" \
  "s_waitcnt lgkmcnt(4)\n\t" \
  "v_mfma_f32_32x32x16_bf16 v[0:15], v[102:105], v[96:99], 0\n\t" \
  "v_mfma_f32_32x32x16_bf16 v[16:31], v[106:109], v[96:99], 0\n\t" \
  "s_nop 7\n\t" \
  "s_nop 1\n\t" \
  "v_mfma_f32_32x32x4_2b_bf16 v[0:31], v[110:111], v[100:101], v[0:31]\n\t" \
  "ds_read_b128 v[102:105], v145 offset:5120\n\t" \
  "ds_read_b128 v[106:109], v145 offset:6144\n\t" \
  "ds_read_b64 v[110:111], v146 offset:5120\n\t" \
  "s_nop 15\n\t" \
  "s_nop 1\n\t" \
  "v_max3_f32 v32, v0, v1, v2\n\t" \
  "v_max3_f32 v33, v3, v4, v5\n\t" \
  "v_max3_f32 v32, v32, v6, v7\n\t" \
  "v_max3_f32 v33, v33, v8, v9\n\t" \
  "v_max3_f32 v32, v32, v10, v11\n\t" \
  "v_max3_f32 v33, v33, v12, v13\n\t" \
  "v_max3_f32 v32, v32, v14, v15\n\t" \
  "v_max3_f32 v33, v33, v16, v17\n\t" \
  "v_max3_f32 v32, v32, v18, v19\n\t" \
  "v_max3_f32 v33, v33, v20, v21\n\t" \
  "v_max3_f32 v32, v32, v22, v23\n\t" \
  "v_max3_f32 v33, v33, v24, v25\n\t" \
  "v_max3_f32 v32, v32, v26, v27\n\t" \
  "v_max3_f32 v33, v33, v28, v29\n\t" \
  "v_max3_f32 v32, v32, v30, v31\n\t" \
  "v_max_f32 v32, v32, v33\n\t" \
  "v_mov_b32 v33, v32\n\t" \
  "s_nop 1\n\t" \
  "v_permlane32_swap_b32 v32, v33\n\t" \
  "s_nop 1\n\t" \
  "v_max_f32 v32, v32, v33\n\t" \
  "v_sub_f32 v80, 0, v32\n\t" \
  "v_sub_f32 v81, 0, v32\n\t" \
  "v_mov_b64 v[82:83], v[80:81]\n\t" \
  "v_mov_b64 v[84:85], v[80:81]\n\t" \
  "v_mov_b64 v[86:87], v[80:81]\n\t" \
  "v_mov_b64 v[88:89], v[80:81]\n\t" \
  "v_mov_b64 v[90:91], v[80:81]\n\t" \
  "v_mov_b64 v[92:93], v[80:81]\n\t" \
  "v_mov_b64 v[94:95], v[80:81]\n\t" \
  "v_pk_add_f32 v[0:1], v[0:1], v[80:81]\n\t" \
  "v_pk_add_f32 v[2:3], v[2:3], v[80:81]\n\t" \
  "v_pk_add_f32 v[4:5], v[4:5], v[80:81]\n\t" \
  "v_pk_add_f32 v[6:7], v[6:7], v[80:81]\n\t" \
  "v_pk_add_f32 v[8:9], v[8:9], v[80:81]\n\t" \
  "v_pk_add_f32 v[10:11], v[10:11], v[80:81]\n\t" \
  "v_pk_add_f32 v[12:13], v[12:13], v[80:81]\n\t" \
  "v_pk_add_f32 v[14:15], v[14:15], v[80:81]\n\t" \
  "v_pk_add_f32 v[16:17], v[16:17], v[80:81]\n\t" \
  "v_pk_add_f32 v[18:19], v[18:19], v[80:81]\n\t" \
  "v_pk_add_f32 v[20:21], v[20:21], v[80:81]\n\t" \
  "v_pk_add_f32 v[22:23], v[22:23], v[80:81]\n\t" \
  "v_pk_add_f32 v[24:25], v[24:25], v[80:81]\n\t" \
  "v_pk_add_f32 v[26:27], v[26:27], v[80:81]\n\t" \
  "v_pk_add_f32 v[28:29], v[28:29], v[80:81]\n\t" \
  "v_pk_add_f32 v[30:31], v[30:31], v[80:81]\n\t" \
  "s_mov_b32 %[it], 0\n\t" \
  "s_waitcnt lgkmcnt(0)\n\t" \
  "s_setprio 2\n\t" \
  "s_barrier\n\t" \
  ".p2align 6\n\t" \
  "1:\n\t" \
  "s_cmp_eq_u32 %[it], 7\n\t" \
  "s_cselect_b32 %[inc], 0xfffdd000, %[c5000]\n\t" \
  "s_bitcmp1_b32 0x62, %[it]\n\t" \
  "s_cbranch_scc1 20f\n\t" \
  "30:\n\t" \
  "s_waitcnt lgkmcnt(4)\n\t" \
  "v_mfma_f32_32x32x16_bf16 v[32:47], v[102:105], v[96:99], v[80:95]\n\t" \
  "v_exp_f32 v0, v0\n\t" \
  "v_exp_f32 v1, v1\n\t" \
  "v_exp_f32 v2, v2\n\t" \
  "v_exp_f32 v3, v3\n\t" \
  "v_smfmac_f32_32x32x32_bf16 v[64:79], v[140:143], v[56:63], v144\n\t" \
  "v_exp_f32 v4, v4\n\t" \
  "v_exp_f32 v5, v5\n\t" \
  "v_exp_f32 v6, v6\n\t" \
  "v_exp_f32 v7, v7\n\t" \
  "v_mfma_f32_32x32x16_bf16 v[48:63], v[106:109], v[96:99], v[80:95]\n\t" \
  "v_exp_f32 v8, v8\n\t" \
  "v_exp_f32 v9, v9\n\t" \
  "v_exp_f32 v10, v10\n\t" \
  "v_exp_f32 v11, v11\n\t" \
  "s_waitcnt lgkmcnt(0)\n\t" \
  "v_smfmac_f32_32x32x32_bf16 v[64:79], v[112:115], v[0:7], v144\n\t" \
  "ds_read_b128 v[102:105], v145 offset:10240\n\t" \
  "ds_read_b128 v[106:109], v145 offset:11264\n\t" \
  "v_exp_f32 v12, v12\n\t" \
  "v_exp_f32 v13, v13\n\t" \
  "v_exp_f32 v14, v14\n\t" \
  "v_exp_f32 v15, v15\n\t" \
  "v_mfma_f32_32x32x4_2b_bf16 v[32:63], v[110:111], v[100:101], v[32:63]\n\t" \
  "ds_read_b64 v[110:111], v146 offset:10240\n\t" \
  "v_exp_f32 v16, v16\n\t" \
  "v_exp_f32 v17, v17\n\t" \
  "v_exp_f32 v18, v18\n\t" \
  "v_exp_f32 v19, v19\n\t" \
  "v_exp_f32 v20, v20\n\t" \
  "v_exp_f32 v21, v21\n\t" \
  "v_exp_f32 v22, v22\n\t" \
  "v_exp_f32 v23, v23\n\t" \
  "v_smfmac_f32_32x32x32_bf16 v[64:79], v[116:119], v[8:15], v144\n\t" \
  "s_mov_b64 exec, %[vmask]\n\t" \
  "ds_read_b128 v[128:131], v147 offset:5120\n\t" \
  "ds_read_b128 v[132:135], v147 offset:5760\n\t" \
  "ds_read_b128 v[136:139], v147 offset:6400\n\t" \
  "ds_read_b128 v[140:143], v147 offset:7040\n\t" \
  "s_mov_b64 exec, -1\n\t" \
  "v_exp_f32 v24, v24\n\t" \
  "v_exp_f32 v25, v25\n\t" \
  "v_exp_f32 v26, v26\n\t" \
  "v_exp_f32 v27, v27\n\t" \
  "v_smfmac_f32_32x32x32_bf16 v[64:79], v[120:123], v[16:23], v144\n\t" \
  "v_exp_f32 v28, v28\n\t" \
  "v_exp_f32 v29, v29\n\t" \
  "v_exp_f32 v30, v30\n\t" \
  "v_exp_f32 v31, v31\n\t" \
  "s_waitcnt lgkmcnt(4)\n\t" \
  "v_mfma_f32_32x32x16_bf16 v[0:15], v[102:105], v[96:99], v[80:95]\n\t" \
  "v_exp_f32 v32, v32\n\t" \
  "v_exp_f32 v33, v33\n\t" \
  "v_exp_f32 v34, v34\n\t" \
  "v_exp_f32 v35, v35\n\t" \
  "v_smfmac_f32_32x32x32_bf16 v[64:79], v[124:127], v[24:31], v144\n\t" \
  "v_exp_f32 v36, v36\n\t" \
  "v_exp_f32 v37, v37\n\t" \
  "v_exp_f32 v38, v38\n\t" \
  "v_exp_f32 v39, v39\n\t" \
  "v_mfma_f32_32x32x16_bf16 v[16:31], v[106:109], v[96:99], v[80:95]\n\t" \
  "v_exp_f32 v40, v40\n\t" \
  "v_exp_f32 v41, v41\n\t" \
  "v_exp_f32 v42, v42\n\t" \
  "v_exp_f32 v43, v43\n\t" \
  "s_waitcnt lgkmcnt(0)\n\t" \
  "v_smfmac_f32_32x32x32_bf16 v[64:79], v[128:131], v[32:39], v144\n\t" \
  "ds_read_b128 v[102:105], v145 offset:15360\n\t" \
  "ds_read_b128 v[106:109], v145 offset:16384\n\t" \
  "v_exp_f32 v44, v44\n\t" \
  "v_exp_f32 v45, v45\n\t" \
  "v_exp_f32 v46, v46\n\t" \
  "v_exp_f32 v47, v47\n\t" \
  "v_mfma_f32_32x32x4_2b_bf16 v[0:31], v[110:111], v[100:101], v[0:31]\n\t" \
  "ds_read_b64 v[110:111], v146 offset:15360\n\t" \
  "v_exp_f32 v48, v48\n\t" \
  "v_exp_f32 v49, v49\n\t" \
  "v_exp_f32 v50, v50\n\t" \
  "v_exp_f32 v51, v51\n\t" \
  "v_exp_f32 v52, v52\n\t" \
  "v_exp_f32 v53, v53\n\t" \
  "v_exp_f32 v54, v54\n\t" \
  "v_exp_f32 v55, v55\n\t" \
  "v_smfmac_f32_32x32x32_bf16 v[64:79], v[132:135], v[40:47], v144\n\t" \
  "s_mov_b64 exec, %[vmask]\n\t" \
  "ds_read_b128 v[112:115], v147 offset:10240\n\t" \
  "ds_read_b128 v[116:119], v147 offset:10880\n\t" \
  "ds_read_b128 v[120:123], v147 offset:11520\n\t" \
  "ds_read_b128 v[124:127], v147 offset:12160\n\t" \
  "s_mov_b64 exec, -1\n\t" \
  "v_exp_f32 v56, v56\n\t" \
  "v_exp_f32 v57, v57\n\t" \
  "v_exp_f32 v58, v58\n\t" \
  "v_exp_f32 v59, v59\n\t" \
  "v_smfmac_f32_32x32x32_bf16 v[64:79], v[136:139], v[48:55], v144\n\t" \
  "v_exp_f32 v60, v60\n\t" \
  "v_exp_f32 v61, v61\n\t" \
  "v_exp_f32 v62, v62\n\t" \
  "v_exp_f32 v63, v63\n\t" \
  "s_bitcmp1_b32 0x81, %[it]\n\t" \
  "s_cbranch_scc1 22f\n\t" \
  "32:\n\t" \
  "v_add_u32 v145, %[inc], v145\n\t" \
  "v_add_u32 v146, %[inc], v146\n\t" \
  "s_waitcnt lgkmcnt(4)\n\t" \
  "v_mfma_f32_32x32x16_bf16 v[32:47], v[102:105], v[96:99], v[80:95]\n\t" \
  "v_exp_f32 v0, v0\n\t" \
  "v_exp_f32 v1, v1\n\t" \
  "v_exp_f32 v2, v2\n\t" \
  "v_exp_f32 v3, v3\n\t" \
  "v_smfmac_f32_32x32x32_bf16 v[64:79], v[140:143], v[56:63], v144\n\t" \
  "v_exp_f32 v4, v4\n\t" \
  "v_exp_f32 v5, v5\n\t" \
  "v_exp_f32 v6, v6\n\t" \
  "v_exp_f32 v7, v7\n\t" \
  "v_mfma_f32_32x32x16_bf16 v[48:63], v[106:109], v[96:99], v[80:95]\n\t" \
  "v_exp_f32 v8, v8\n\t" \
  "v_exp_f32 v9, v9\n\t" \
  "v_exp_f32 v10, v10\n\t" \
  "v_exp_f32 v11, v11\n\t" \
  "s_waitcnt lgkmcnt(0)\n\t" \
  "v_smfmac_f32_32x32x32_bf16 v[64:79], v[112:115], v[0:7], v144\n\t" \
  "ds_read_b128 v[102:105], v145 offset:0\n\t" \
  "ds_read_b128 v[106:109], v145 offset:1024\n\t" \
  "v_exp_f32 v12, v12\n\t" \
  "v_exp_f32 v13, v13\n\t" \
  "v_exp_f32 v14, v14\n\t" \
  "v_exp_f32 v15, v15\n\t" \
  "v_mfma_f32_32x32x4_2b_bf16 v[32:63], v[110:111], v[100:101], v[32:63]\n\t" \
  "ds_read_b64 v[110:111], v146 offset:0\n\t" \
  "v_exp_f32 v16, v16\n\t" \
  "v_exp_f32 v17, v17\n\t" \
  "v_exp_f32 v18, v18\n\t" \
  "v_exp_f32 v19, v19\n\t" \
  "v_exp_f32 v20, v20\n\t" \
  "v_exp_f32 v21, v21\n\t" \
  "v_exp_f32 v22, v22\n\t" \
  "v_exp_f32 v23, v23\n\t" \
  "v_smfmac_f32_32x32x32_bf16 v[64:79], v[116:119], v[8:15], v144\n\t" \
  "s_mov_b64 exec, %[vmask]\n\t" \
  "ds_read_b128 v[128:131], v147 offset:15360\n\t" \
  "ds_read_b128 v[132:135], v147 offset:16000\n\t" \
  "ds_read_b128 v[136:139], v147 offset:16640\n\t" \
  "ds_read_b128 v[140:143], v147 offset:17280\n\t" \
  "s_mov_b64 exec, -1\n\t" \
  "v_exp_f32 v24, v24\n\t" \
  "v_exp_f32 v25, v25\n\t" \
  "v_exp_f32 v26, v26\n\t" \
  "v_exp_f32 v27, v27\n\t" \
  "v_smfmac_f32_32x32x32_bf16 v[64:79], v[120:123], v[16:23], v144\n\t" \
  "v_exp_f32 v28, v28\n\t" \
  "v_exp_f32 v29, v29\n\t" \
  "v_exp_f32 v30, v30\n\t" \
  "v_exp_f32 v31, v31\n\t" \
  "s_bitcmp1_b32 1, %[it]\n\t" \
  "s_cbranch_scc1 23f\n\t" \
  "33:\n\t" \
  "v_add_u32 v147, %[inc], v147\n\t" \
  "s_waitcnt lgkmcnt(4)\n\t" \
  "v_mfma_f32_32x32x16_bf16 v[0:15], v[102:105], v[96:99], v[80:95]\n\t" \
  "v_exp_f32 v32, v32\n\t" \
  "v_exp_f32 v33, v33\n\t" \
  "v_exp_f32 v34, v34\n\t" \
  "v_exp_f32 v35, v35\n\t" \
  "v_smfmac_f32_32x32x32_bf16 v[64:79], v[124:127], v[24:31], v144\n\t" \
  "v_exp_f32 v36, v36\n\t" \
  "v_exp_f32 v37, v37\n\t" \
  "v_exp_f32 v38, v38\n\t" \
  "v_exp_f32 v39, v39\n\t" \
  "v_mfma_f32_32x32x16_bf16 v[16:31], v[106:109], v[96:99], v[80:95]\n\t" \
  "v_exp_f32 v40, v40\n\t" \
  "v_exp_f32 v41, v41\n\t" \
  "v_exp_f32 v42, v42\n\t" \
  "v_exp_f32 v43, v43\n\t" \
  "s_waitcnt lgkmcnt(0)\n\t" \
  "v_smfmac_f32_32x32x32_bf16 v[64:79], v[128:131], v[32:39], v144\n\t" \
  "ds_read_b128 v[102:105], v145 offset:5120\n\t" \
  "ds_read_b128 v[106:109], v145 offset:6144\n\t" \
  "v_exp_f32 v44, v44\n\t" \
  "v_exp_f32 v45, v45\n\t" \
  "v_exp_f32 v46, v46\n\t" \
  "v_exp_f32 v47, v47\n\t" \
  "v_mfma_f32_32x32x4_2b_bf16 v[0:31], v[110:111], v[100:101], v[0:31]\n\t" \
  "ds_read_b64 v[110:111], v146 offset:5120\n\t" \
  "v_exp_f32 v48, v48\n\t" \
  "v_exp_f32 v49, v49\n\t" \
  "v_exp_f32 v50, v50\n\t" \
  "v_exp_f32 v51, v51\n\t" \
  "v_exp_f32 v52, v52\n\t" \
  "v_exp_f32 v53, v53\n\t" \
  "v_exp_f32 v54, v54\n\t" \
  "v_exp_f32 v55, v55\n\t" \
  "v_smfmac_f32_32x32x32_bf16 v[64:79], v[132:135], v[40:47], v144\n\t" \
  "s_mov_b64 exec, %[vmask]\n\t" \
  "ds_read_b128 v[112:115], v147 offset:0\n\t" \
  "ds_read_b128 v[116:119], v147 offset:640\n\t" \
  "ds_read_b128 v[120:123], v147 offset:1280\n\t" \
  "ds_read_b128 v[124:127], v147 offset:1920\n\t" \
  "s_mov_b64 exec, -1\n\t" \
  "v_exp_f32 v56, v56\n\t" \
  "v_exp_f32 v57, v57\n\t" \
  "v_exp_f32 v58, v58\n\t" \
  "v_exp_f32 v59, v59\n\t" \
  "v_smfmac_f32_32x32x32_bf16 v[64:79], v[136:139], v[48:55], v144\n\t" \
  "v_exp_f32 v60, v60\n\t" \
  "v_exp_f32 v61, v61\n\t" \
  "v_exp_f32 v62, v62\n\t" \
  "v_exp_f32 v63, v63\n\t" \
  "s_add_u32 %[it], %[it], 1\n\t" \
  "s_cmp_lt_u32 %[it], 8\n\t" \
  "s_cbranch_scc1 1b\n\t" \
  "s_branch 40f\n\t" \
  "20:\n\t" \
  "s_cmp_eq_u32 %[it], 1\n\t" \
  "s_cbranch_scc0 50f\n\t" \
  "s_barrier\n\t" \
  "s_setprio 2\n\t" \
  "s_branch 30b\n\t" \
  "50:\n\t" \
  "s_cmp_eq_u32 %[it], 5\n\t" \
  "s_cbranch_scc0 51f\n\t" \
  "s_mov_b64 exec, %[wbit]\n\t" \
  "ds_write_b64 %[sig], %[tag]\n\t" \
  "s_mov_b64 exec, -1\n\t" \
  "s_branch 30b\n\t" \
  "51:\n\t" \
  "s_setprio 1\n\t" \
  "s_branch 30b\n\t" \
  "22:\n\t" \
  "s_cmp_eq_u32 %[it], 0\n\t" \
  "s_cbranch_scc0 70f\n\t" \
  "s_setprio 1\n\t" \
  "s_branch 32b\n\t" \
  "70:\n\t" \
  "s_setprio 0\n\t" \
  "s_branch 32b\n\t" \
  "23:\n\t" \
  "s_setprio 0\n\t" \
  "s_branch 33b\n\t" \
  "40:\n\t" \
  "v_smfmac_f32_32x32x32_bf16 v[64:79], v[140:143], v[56:63], v144\n\t" \
  "s_waitcnt lgkmcnt(0)\n\t" \
  "s_setprio 0\n\t" \
  "s_nop 11\n\t"
#define ATTN_CLOB "v0","v1","v2","v3","v4","v5","v6","v7","v8","v9","v10","v11","v12","v13","v14","v15","v16","v17","v18","v19","v20","v21","v22","v23","v24","v25","v26","v27","v28","v29","v30","v31","v32","v33","v34","v35","v36","v37","v38","v39","v40","v41","v42","v43","v44","v45","v46","v47","v48","v49","v50","v51","v52","v53","v54","v55","v56","v57","v58","v59","v60","v61","v62","v63","v80","v81","v82","v83","v84","v85","v86","v87","v88","v89","v90","v91","v92","v93","v94","v95","v96","v97","v98","v99","v100","v101","v102","v103","v104","v105","v106","v107","v108","v109","v110","v111","v112","v113","v114","v115","v116","v117","v118","v119","v120","v121","v122","v123","v124","v125","v126","v127","v128","v129","v130","v131","v132","v133","v134","v135","v136","v137","v138","v139","v140","v141","v142","v143","v144"

DI unsigned kr_off(int lane) { return 2048 + (lane & 31) * 16 + (((lane >> 5) ^ (lane >> 4)) & 1) * 8; }
DI void attn_slow(const char* kvp, const char* qp, int t, int lane, float (&o)[12]) {
  const bf16x8 q = *(const bf16x8*)(qp + t * 1024 + lane * 16);
  const s16x4 qr = *(const s16x4*)(qp + 2048 + (lane & 31) * 16 + t * 8);
  const bool vl = (lane & 31) < 20;
  const unsigned one = ((lane & 31) == 20) ? 0x3F803F80u : 0u;
  f32x16 O = {};
  float m = -__builtin_inff();
#pragma unroll 1
  for (int u = 0; u < 32; ++u) {
    const char* base = kvp + u * 5120;
    f32x32 s = __builtin_amdgcn_mfma_f32_32x32x4bf16_1k(*(const s16x4*)(base + kr_off(lane)), qr, (f32x32){}, 0, 0, 0);
#pragma unroll
    for (int t = 0; t < 2; ++t) {
      f32x16 c;
#pragma unroll
      for (int i = 0; i < 16; ++i) c[i] = s[16 * t + i];
      c = MFMA32(*(const bf16x8*)(base + t * 1024 + lane * 16), q, c);
      float mx = m;
#pragma unroll
      for (int i = 0; i < 16; ++i) mx = fmaxf(mx, c[i]);
      mx = fmaxf(mx, __shfl_xor(mx, 32));
      const float al = __builtin_amdgcn_exp2f(m - mx);
      m = mx;
#pragma unroll
      for (int i = 0; i < 16; ++i) { O[i] *= al; c[i] = __builtin_amdgcn_exp2f(c[i] - mx); }
#pragma unroll
      for (int g = 0; g < 2; ++g) {
        u32x4 v = {one, one, one, one};
        if (vl) v = *(const u32x4*)(base + 2560 + (2 * t + g) * 640 + (lane & 31) * 32 + (((lane >> 5) ^ (lane >> 4)) & 1) * 16);
        f32x8 p;
#pragma unroll
        for (int i = 0; i < 8; ++i) p[i] = c[8 * g + i];
        O = __builtin_amdgcn_smfmac_f32_32x32x32_bf16(__builtin_bit_cast(bf16x8, v), __builtin_bit_cast(bf16x16, p), O, 0xdddd, 0, 0);
      }
    }
  }
#pragma unroll
  for (int i = 0; i < 12; ++i) o[i] = O[i];
}
template <int KB> DI void w_fill(unsigned dst, const char* src, int lane) {
#pragma unroll 2
  for (int i = 0; i < KB / 4; ++i) { dma<4>(dst + i * 4096, lane * 16, src + i * 4096); waitv<56>(); }
}
DI bf16x8 lda(rsrc_t r, unsigned l16, unsigned base, int k) { return __builtin_bit_cast(bf16x8, __builtin_amdgcn_raw_buffer_load_b128(r, l16 + (k & 3) * 1024, base + (k >> 2) * 4096, ASC)); }
template <int N, int I = 0> DI void ilv1() { if constexpr (I < N) { SGB(0x100, 1); if constexpr (I == 0) SGB(0x20, 1); SGB(0x8, 1); ilv1<N, I + 1>(); } }
template <int I = 0> DI void ilv2() { if constexpr (I < 7) { SGB(0x8, 1); SGB(I < 5 ? 0x100 : 0x20, 1); ilv2<I + 1>(); } else SGB(0x8, 3); }
template <int N> DI void waitl() { __builtin_amdgcn_s_waitcnt(0xC07F | (N << 8)); }
constexpr int h1k(int k) { return k < 16 ? 3 * (k >> 1) + (k & 1) : 3 * (k - 16) + 2; }
template <int KH, int DA> DI void n80_loop(rsrc_t rw, unsigned ab0, unsigned ab1, unsigned wb, unsigned wb8, int lane, f32x4 (&acc)[2][5], char* ws, u64 tag, int word) {
  bf16x8 a[2][DA + 1], w[2][5];
  const unsigned l16 = lane * 16;
  bar();
#pragma unroll
  for (int k = 0; k < DA; ++k) { a[0][k] = lda(rw, l16, ab0, h1k(k)); a[1][k] = lda(rw, l16, ab1, h1k(k)); SB(); }
  waitv<2 * DA>();
  if (lane == 0) __hip_atomic_store((gu64*)(ws + WS_FL) + word, tag, __ATOMIC_RELAXED, __HIP_MEMORY_SCOPE_AGENT);
#pragma unroll
  for (int c = 0; c < 5; ++c) w[0][c] = ldsr128(wb + c * 1024);
  SB();
#pragma unroll
  for (int k = 0; k < KH; ++k) {
    if (k == KB1 || k == KB2 || k == 19) bar();
    if (W1W) { waitl<0>(); SB(); }
    if (k + 1 < KH) {
#pragma unroll
      for (int c = 0; c < 5; ++c) w[(k + 1) & 1][c] = ldsr128((((k + 1) & 8) ? wb8 : wb) + ((k + 1) & 7) * 5120 + c * 1024);
    }
    if (k + DA < KH) { a[0][(k + DA) % (DA + 1)] = lda(rw, l16, ab0, h1k(k + DA)); a[1][(k + DA) % (DA + 1)] = lda(rw, l16, ab1, h1k(k + DA)); }
#pragma unroll
    for (int c = 0; c < 5; ++c) { acc[1][c] = MFMA16(w[k & 1][c], a[1][k % (DA + 1)], acc[1][c]); acc[0][c] = MFMA16(w[k & 1][c], a[0][k % (DA + 1)], acc[0][c]); }
    ilv2(); SB();
  }
}
template <int OFF> DI void wr4(unsigned m, const u32x4& v) {
  asm volatile("s_mov_b32 m0, %0\n\ts_nop 0\n\tds_write_addtid_b32 %1 offset:%5\n\tds_write_addtid_b32 %2 offset:%6\n\tds_write_addtid_b32 %3 offset:%7\n\tds_write_addtid_b32 %4 offset:%8"
               ::"s"(m), "v"(v[0]), "v"(v[1]), "v"(v[2]), "v"(v[3]), "n"(OFF), "n"(OFF + 256), "n"(OFF + 512), "n"(OFF + 768) : "memory");
}
template <int I0, int I1> DI void dump(unsigned m, const u32x4 (&st)[40]) { if constexpr (I0 < I1) { wr4<I0 * 1024>(m, st[I0]); dump<I0 + 1, I1>(m, st); } }
DI void n80_join(unsigned L0, int rb, f32x4 (&acc2)[2][5]) {
  f32x4 (&acc)[5] = acc2[0];
  {
    f32x4 (&o)[5] = acc2[1];
    const unsigned m = L0 + 46080 + (rb ^ 1) * 5120;
    asm volatile("s_nop 7\n\ts_nop 1" : "+v"(o[0]), "+v"(o[1]), "+v"(o[2]), "+v"(o[3]), "+v"(o[4]));
    wr4<56320>(m, __builtin_bit_cast(u32x4, o[0])); wr4<57344>(m, __builtin_bit_cast(u32x4, o[1])); wr4<58368>(m, __builtin_bit_cast(u32x4, o[2]));
    wr4<59392>(m, __builtin_bit_cast(u32x4, o[3])); wr4<60416>(m, __builtin_bit_cast(u32x4, o[4]));
  }
  waitl_bar();
  {
    const unsigned m = L0 + 46080 + rb * 5120;
    float t[20];
#define RD(i) "ds_read_addtid_b32 %" #i " offset:56320+256*" #i "\n\t"
    asm volatile("s_mov_b32 m0, %20\n\ts_nop 0\n\t" RD(0) RD(1) RD(2) RD(3) RD(4) RD(5) RD(6) RD(7) RD(8) RD(9) RD(10) RD(11) RD(12) RD(13) RD(14) RD(15) RD(16) RD(17) RD(18) RD(19) "s_waitcnt lgkmcnt(0)"
                 : "=v"(t[0]), "=v"(t[1]), "=v"(t[2]), "=v"(t[3]), "=v"(t[4]), "=v"(t[5]), "=v"(t[6]), "=v"(t[7]), "=v"(t[8]), "=v"(t[9]), "=v"(t[10]), "=v"(t[11]), "=v"(t[12]), "=v"(t[13]),
                   "=v"(t[14]), "=v"(t[15]), "=v"(t[16]), "=v"(t[17]), "=v"(t[18]), "=v"(t[19]) : "s"(m) : "memory");
#undef RD
#pragma unroll
    for (int i = 0; i < 20; ++i) acc[i >> 2][i & 3] += t[i];
  }
}

__global__ void __launch_bounds__(768) mega_k(const char* __restrict__ x, const char* __restrict__ wqkv, const char* __restrict__ wo, const char* __restrict__ w1, const char* __restrict__ w2, char* __restrict__ ws, char* __restrict__ out) {
  extern __shared__ __attribute__((aligned(16))) char lds[];
  const int lane = threadIdx.x & 63, wid = __builtin_amdgcn_readfirstlane(threadIdx.x >> 6);
  qkv_phase(x, wqkv, wo, w1, ws, lds, lane, wid);
  const u64 tag = get_tag(ws);
  waitv_bar<0>();
  const int b = blockIdx.x, xc = b & 7, j = b >> 3, h = xc * 4 + (j & 3), qt = j >> 2;
  const int rt = MAP ? (xc >> 1) * 8 + (j & 7) : xc * 4 + (j & 3), ct = MAP ? (xc & 1) * 4 + (j >> 3) : j >> 2;
  const int rt1 = rt >> 1, ct1 = ct * 2 + (j & 1);
  const int mate = MAP ? (xc & 6) + ((lane >> 2) & 1) + 8 * ((j & 6) + ((lane >> 3) & 1) + 8 * (lane >> 4)) : xc + 8 * ((j & 2) + ((lane >> 2) & 1) + 4 * (lane >> 3));
  const unsigned L0 = (unsigned)(size_t)lds;
  const char* kvp = ws + WS_KV + (size_t)h * 163840;
  if (wid == 8) {
    __builtin_amdgcn_s_setprio(3);
    const unsigned voff = lane * 16;
    publish(ws, tag, b * 192 + 131, lane);
    __builtin_amdgcn_s_sleep(SL0);
    await<SLQ>(ws, tag, qkv_wg(lane & 15, (640 + (lane >> 5) * 640 + 20 * h + ((lane >> 4) & 1) * 19) >> 7) * 192 + 131, qkv_wg(2 * qt + (lane & 1), (20 * h + ((lane >> 1) & 1) * 19) >> 7) * 192 + 131);
    dmac<4>(L0, voff, kvp);
    bar();
#pragma unroll
    for (int i = 1; i < 12; ++i) dmac<4>(L0 + i * 4096, voff, kvp + i * 4096);
    waitv_bar<36>();
#pragma unroll
    for (int i = 12; i < 18; ++i) dmac<4>(L0 + i * 4096, voff, kvp + i * 4096);
    waitv_bar<40>();
#pragma unroll 2
    for (int i = 18; i < 40; ++i) { dmac<4>(L0 + i * 4096, voff, kvp + i * 4096); waitv<56>(); }
    waitv_bar<0>();
    await<8>(ws, tag, qkv_wg(2 * ct + (lane & 1), 15) * 192 + 131);
    const char* p1 = ws + WS_W1 + (size_t)ct1 * 122880;
    const char* p2 = ws + WS_W2 + (size_t)ct * 245760;
    {
      unsigned o = lane < 32 ? (unsigned)WS_O + (qt * 16 + (lane >> 1)) * 20480 + 5 * (h >> 1) * 256 + 1024 + (lane & 1) * 128 : (unsigned)WS_ST + ct * 16384 + rt * 512 + (lane & 3) * 128;
      asm volatile("s_sleep 44\n\tglobal_load_dword %0, %0, %1 sc1\n\ts_sleep 26\n\ts_waitcnt vmcnt(0)" : "+v"(o) : "s"(ws) : "memory");
    }
    u32x4 st[W8 ? W8 : 1];
    if (W8) {
      const int g1 = ct1 * 24 + 16 + (lane & 7);
      await<8>(ws, tag, g1 < 256 ? g1 * 192 + 176 : (g1 - 128) * 192 + 160);
      const rsrc_t rs = mk_rsrc(p1 + 122880 - W8 * 1024, W8 * 1024);
#pragma unroll
      for (int i = 0; i < W8; ++i) st[i] = __builtin_amdgcn_raw_buffer_load_b128(rs, voff + (i & 3) * 1024, (i >> 2) * 4096, 0);
    }
#pragma unroll 1
    for (unsigned spin = 0; spin < (1u << 20); ++spin) {
      u64 v;
      asm volatile("ds_read_b64 %0, %1\n\ts_waitcnt lgkmcnt(0)" : "=v"(v) : "v"(L0 + (lane & 7) * 8) : "memory");
      if (__all(v == tag)) break;
      __builtin_amdgcn_s_sleep(8);
    }
    w_fill<100>(L0, ws + WS_WO + (size_t)ct * 102400, lane);
#pragma unroll
    for (int i = 0; i < W8; ++i) asm volatile("" : "+v"(st[i]));
    waitv_bar<0>();
    publish(ws, tag, b * 192 + 128, lane);
    w_fill<40 - W8>(L0 + 122880, p1 + 81920, lane);
    dma<3>(L0 + 118784, lane * 16, p2 + 77824);
    dma<1>(L0 + 121856, lane * 16, ws + WS_CS + ct1 * 384);
    u32x4 last = *(const u32x4*)(p2 + 80896 + lane * 16);
#pragma unroll
    for (int i = 0; i < W8; ++i) *(LAS u32x4*)(size_t)(L0 + 163840 - W8 * 1024 + voff + i * 1024) = st[i];
    asm volatile("s_waitcnt vmcnt(0) lgkmcnt(0)" ::: "memory");
    asm volatile("" : "+v"(last));
    __builtin_amdgcn_s_sleep(SL1);
    await_panel<MAP ? 20 : 10>(ws, tag, ((rt >> 2) * 32 + (lane & 31)) * 192 + 128, ws + WS_O + rt * 81920 + (MAP ? (ct & 3) * 20480 : ct * 10240), lane);
    bar();
    w_fill<40>(L0 + 40960, p1 + 40960, lane);
    w_fill<XS>(L0 + 81920, p2 + 40960, lane);
    waitv<0>();
    __builtin_amdgcn_s_sleep(SL2);
    await_panel<MAP ? 20 : 10>(ws, tag, mate * 192 + (4 + (lane & 3)) * 16, ws + WS_H + rt1 * 163840 + (MAP ? (ct1 & 7) * 20480 : ct1 * 10240), lane);
    bar();
    w_fill<40>(L0, p2, lane); w_fill<XG - 40>(L0 + 40960, p2 + 122880, lane);
    bar();
    w_fill<80 - XG>(L0 + XG * 1024, p2 + 81920 + XG * 1024, lane);
    w_fill<36 - XS>(L0 + 81920 + XS * 1024, p2 + 40960 + XS * 1024, lane);
    *(LAS u32x4*)(size_t)(L0 + 121856 + lane * 16) = last;
    waitv<116 - XG - XS>();
    bar();
    asm volatile("s_waitcnt vmcnt(0) lgkmcnt(0)" ::: "memory");
    bar();
    __builtin_amdgcn_s_sleep(SL4);
    await<2>(ws, tag, mate * 192 + (4 * (rt & 1) + (lane & 3)) * 16 + 1);
    bar();
    __builtin_amdgcn_endpgm();
  }
  if (wid > 8) {
    __builtin_amdgcn_s_setprio(3);
    bar(); bar(); bar(); bar();
    {
      const int i16 = lane & 15, kg = lane >> 4;
      const unsigned l16 = opq(lane * 16);
      const rsrc_t r1 = mk_rsrc(w1, 1966080), r2 = mk_rsrc(w2, 1966080), rd = mk_rsrc(ws + WS_W1, 2 * 1966080);
      const int t = wid - 9;
      {
        u32x4 f[5];
        if (t == 0 || (t == 1 && b < 128)) {
          const int grp = t ? 256 + b : b, c = grp / 48, ks = grp - c * 48;
          const int kq = ks >= 24 ? ks - 24 : ks, kf = ks - kq + (kq < 16 ? 3 * (kq >> 1) + (kq & 1) : 3 * (kq - 16) + 2);
          const bool tr = kq >= 16;
          const unsigned lo = tr ? (20 * (lane >> 4) + ((lane >> 2) & 3)) * 2 + (16 * ((lane >> 1) & 1) + 2 * (lane & 1)) * 1280 : (20 * (i16 >> 2) + (i16 & 3)) * 2 + 4 * kg * 1280;
          const unsigned src = kf * 32 * 1280 + c * 160, o0 = opq(l16 + 1966080 + grp * 5120), o1 = opq(o0 + 4096);
#pragma unroll
          for (int cb = 0; cb < 5; ++cb) f[cb] = gather8<1280>(r2, src + cb * 8, lo, lo + (tr ? 8 : 16) * 1280, tr ? 5120 : 2560);
#pragma unroll
          for (int cb = 0; cb < 5; ++cb) st16(f[cb], rd, (cb < 4 ? o0 : o1) + (cb & 3) * 1024);
        } else {
          const int grp = t == 2 ? b : 128 + b, q = grp >> 2, m = grp & 3, c = q / 6, cb = q - c * 6;
          const unsigned src = m * 160 * 3072 + q * 32;
#pragma unroll
          for (int k = 0; k < 5; ++k) {
            const int ch = 4 * k + kg, c10 = ch >= 10 ? ch - 10 : ch;
            const unsigned lo = ((ch >= 10 ? 80 : 0) + (c10 < 8 ? 20 * (c10 >> 1) + 8 * (c10 & 1) : 40 * (c10 - 8) + 16)) * 3072 + i16 * 2;
            f[k] = gather8<3072>(r1, src, lo, lo + (c10 < 8 ? 4 : 20) * 3072);
          }
          const unsigned o0 = l16 + c * 122880 + (cb < 4 ? cb * 1024 + m * 20480 : 81920 + (cb - 4) * 1024 + m * 10240), st = cb < 4 ? 4096 : 2048;
#pragma unroll
          for (int k = 0; k < 5; ++k) st16(f[k], rd, o0 + k * st);
        }
      }
      publish(ws, tag, b * 192 + wid * 16, lane);
    }
    __builtin_amdgcn_s_sleep(SLP);
    { const int g2 = ct * 48 + (lane < 48 ? lane : 0), g1 = ct1 * 24 + (lane < 24 ? lane : 0);
      await<8>(ws, tag, g2 < 256 ? g2 * 192 + 144 : (g2 - 256) * 192 + 160, lane == 63 ? qkv_wg(ct1, 15) * 192 + 131 : g1 < 256 ? g1 * 192 + 176 : (g1 - 128) * 192 + 160); }
    if (b == 0 && wid == 11) {
      await<8>(ws, tag, lane * 192 + 131, (64 + lane) * 192 + 131); await<8>(ws, tag, (128 + lane) * 192 + 131, (192 + lane) * 192 + 131);
      if (lane == 0) __hip_atomic_store((gu64*)(ws + WS_EP), tag, __ATOMIC_RELAXED, __HIP_MEMORY_SCOPE_AGENT);
    }
    const rsrc_t rs = mk_rsrc(ws + WS_W2 + (size_t)ct * 245760 + (wid == 9 ? 81920 : wid == 10 ? 204800 : 163840), 40960);
    const unsigned l16 = opq(lane * 16);
    u32x4 st[40];
#pragma unroll
    for (int i = 0; i < 40; ++i) st[i] = __builtin_amdgcn_raw_buffer_load_b128(rs, l16 + (i & 3) * 1024, (i >> 2) * 4096, 0);
    {
      const char* pb[4];
#pragma unroll
      for (int i = 0; i < 4; ++i) {
        const int r = min(wid == 9 ? (j & 3) * 4 + i : (j & 2) * 4 + (wid - 10) * 4 + i, 14);
        pb[i] = ws + (r * 8192 + (wid == 9 ? (unsigned)WS_W2 + ct * 245760 + (r < 10 ? 0 : 40960) : (unsigned)WS_W1 + ct1 * 122880));
      }
      unsigned o = wid == 9 ? lane * 1280 : (unsigned)WS_CS + ct1 * 384 + (lane & 7) * 128;
      unsigned o1 = lane * 128, t;
#define B6 "s_barrier\n\ts_barrier\n\ts_barrier\n\ts_barrier\n\ts_barrier\n\ts_barrier\n\ts_waitcnt vmcnt(0)"
#define T4 "global_load_dword %0, %1, %3\n\tglobal_load_dword %0, %1, %4\n\tglobal_load_dword %0, %1, %5\n\tglobal_load_dword %0, %1, %6\n\tglobal_load_dword %0, %2, %7\n\t"
      if (wid == 9) asm volatile(T4 "global_load_dword %0, %2, %7 offset:156\n\tglobal_load_dword %0, %2, %7 offset:80\n\t" B6
                                 : "=&v"(t) : "v"(o1), "v"(o), "s"(pb[0]), "s"(pb[1]), "s"(pb[2]), "s"(pb[3]), "s"(x + (size_t)rt * 81920 + ct * 160) : "memory");
      else asm volatile(T4 B6 : "=&v"(t) : "v"(o1), "v"(o), "s"(pb[0]), "s"(pb[1]), "s"(pb[2]), "s"(pb[3]), "s"(ws) : "memory");
#undef T4
#undef B6
    }
    if (wid == 11) {
#pragma unroll
      for (int i = 0; i < 40; ++i) { if (i == D11) bar(); *(LAS u32x4*)(size_t)(L0 + 122880 + l16 + i * 1024) = st[i]; }
    } else {
      const unsigned m = L0 + (wid - 9) * 40960;
      if (wid == 9) { await<1>(ws, tag, mate * 192 + (4 * (rt & 1) + (lane & 3)) * 16 + 2); }
      bar(); bar();
      dump<0, ND>(m, st);
      waitl_bar();
      dump<20, 20 + ND>(m, st);
    }
    waitl_bar();
    __builtin_amdgcn_endpgm();
  }
  const rsrc_t rw = mk_rsrc(ws, (unsigned)WS_END);
  {
    const char* qp = ws + WS_Q + (size_t)(h * 32 + qt * 4 + (wid >> 1)) * 2560;
    unsigned qoff = (wid & 1) * 1024 + lane * 16, qroff = 2048 + (lane & 31) * 16 + (wid & 1) * 8;
    unsigned ak = L0 + lane * 16, akr = L0 + kr_off(lane), av = L0 + 2560 + (lane & 31) * 32 + (((lane >> 5) ^ (lane >> 4)) & 1) * 16;
    unsigned long long ones = ((lane & 31) == 20) ? 0x3F803F803F803F80ull : 0ull;
    unsigned c5000 = 0x5000;
    unsigned long long vmask = 0x000FFFFF000FFFFFull;
    f32x16 O; unsigned it, inc;
    bar();
    asm volatile(ATTN_ASM
      : "=&{v[64:79]}"(O), "+{v145}"(ak), "+{v146}"(akr), "+{v147}"(av), [it] "=&s"(it), [inc] "=&s"(inc)
      : [qp] "s"(qp), [qoff] "v"(qoff), [qroff] "v"(qroff), [ones] "v"(ones), [vmask] "s"(vmask), [c5000] "s"(c5000), [wbit] "s"(1ull << wid), [sig] "v"(opq(L0 + lane * 8)), [tag] "v"(tag)
      : ATTN_CLOB, "memory", "scc");
    float o[12];
#pragma unroll
    for (int i = 0; i < 12; ++i) o[i] = O[i];
    float t = 0.f;
#pragma unroll
    for (int i = 0; i < 12; ++i) t += __builtin_fabsf(o[i]);
    if (__builtin_expect(__any(!(t <= 3.0e38f)), 0)) attn_slow(kvp, qp, wid & 1, lane, o);
    auto sw = __builtin_amdgcn_permlane32_swap(__float_as_uint(o[8]), __float_as_uint(o[8]), false, false);
    const float rl = __builtin_amdgcn_rcpf(__uint_as_float(sw[1]));
    const unsigned ob = WS_O + (qt * 16 + wid * 2) * 20480 + (5 * (h >> 1)) * 256, ro = ((lane >> 4) & 1) * 20480 + (lane & 15) * 16;
    u32x4 w; w[0] = cvtpk(o[0] * rl, o[1] * rl); w[1] = cvtpk(o[2] * rl, o[3] * rl); w[2] = cvtpk(o[4] * rl, o[5] * rl); w[3] = cvtpk(o[6] * rl, o[7] * rl);
    st16(w, rw, ob + ro + (lane >> 5) * 256 + (h & 1) * 512);
    if (lane < 32) { u32x2 w2; w2[0] = cvtpk(o[8] * rl, o[9] * rl); w2[1] = cvtpk(o[10] * rl, o[11] * rl); st8(w2, rw, ob + ro + 1024 + 8 * (h & 1)); }
    waitv<0>();
    bar();
  }
  const int rb = wid & 3, kh = wid & 1, tok = rt * 64 + rb * 16 + (lane & 15), g = lane >> 4;
  unsigned hr[10] = {};
  u32x2 q0, q1;
  if (wid < 4) {
    const rsrc_t rs = mk_rsrc(ws + WS_W1 + (size_t)ct1 * 122880 + wid * 10240, 10240);
    const unsigned l16 = lane * 16;
    u32x4 st[10];
    if (F1) { bar(); __builtin_amdgcn_s_sleep(SLH); }
#pragma unroll
    for (int i = 0; i < 10; ++i) st[i] = __builtin_amdgcn_raw_buffer_load_b128(rs, l16 + (i & 3) * 1024, (i >> 2) * 4096, 0);
    if (!F1) bar();
    bar();
#pragma unroll
    for (int i = 0; i < 10; ++i) *(LAS u32x4*)(size_t)(L0 + wid * 10240 + l16 + i * 1024) = st[i];
    asm volatile("s_waitcnt lgkmcnt(0)" ::: "memory");
  } else {
    const char* xp = x + (size_t)tok * 1280 + ct * 160 + g * 40;
    const u32x4 r0 = *(const u32x4*)xp, r1 = *(const u32x4*)(xp + 16); const u32x2 r2 = *(const u32x2*)(xp + 32);
    f32x4 acc[5] = {{bflo(r0[0]), bfhi(r0[0]), bflo(r0[1]), bfhi(r0[1])}, {bflo(r0[2]), bfhi(r0[2]), bflo(r0[3]), bfhi(r0[3])}, {bflo(r1[0]), bfhi(r1[0]), bflo(r1[1]), bfhi(r1[1])},
                    {bflo(r1[2]), bfhi(r1[2]), bflo(r1[3]), bfhi(r1[3])}, {bflo(r2[0]), bfhi(r2[0]), bflo(r2[1]), bfhi(r2[1])}};
    {
      bf16x8 a[DA0 + 1], w[3][5];
      const unsigned l16 = lane * 16, ab = WS_O + (rt * 4 + rb) * 20480, wb = opq(L0 + l16), wb1 = opq(L0 + 51200 + l16);
      auto rdw = [&](int k) {
#pragma unroll
        for (int c = 0; c < 5; ++c) w[k % 3][c] = ldsr128((k < 10 ? wb + k * 5120 : wb1 + (k - 10) * 5120) + c * 1024); };
      rdw(0); rdw(1);
      asm volatile("" : "+v"(acc[0]), "+v"(acc[1]), "+v"(acc[2]), "+v"(acc[3]), "+v"(acc[4]));
      bar();
#pragma unroll
      for (int k = 0; k < DA0; ++k) { a[k] = lda(rw, l16, ab, k); SB(); }
#pragma unroll
      for (int k = 0; k < 20; ++k) {
        if (W1W) { if (k < 19) waitl<5>(); else waitl<0>(); SB(); }
        if (k + 2 < 20) rdw(k + 2);
        if (k + DA0 < 20) a[(k + DA0) % (DA0 + 1)] = lda(rw, l16, ab, k + DA0);
#pragma unroll
        for (int c = 0; c < 5; ++c) acc[c] = MFMA16(w[k % 3][c], a[k % (DA0 + 1)], acc[c]);
        SGB(0x8, 1); SGB(0x100, 1); SGB(0x8, 1); SGB(0x100, 1); SGB(0x8, 1); SGB(0x100, 1); SGB(0x8, 1); SGB(0x100, 1); SGB(0x20, 1); SGB(0x8, 1); SGB(0x100, 1); SB();
      }
    }
#pragma unroll
    for (int c = 0; c < 5; ++c) { hr[2 * c] = cvtpk(acc[c][0], acc[c][1]); hr[2 * c + 1] = cvtpk(acc[c][2], acc[c][3]); }
    const unsigned hb = WS_H + (rt * 4 + rb) * 20480 + ct * 2560 + (lane & 15) * 16, h0 = opq(hb + g * 512);
    st16((u32x4){hr[0], hr[1], hr[2], hr[3]}, rw, h0);
    st16((u32x4){hr[4], hr[5], hr[6], hr[7]}, rw, h0 + 256);
    {
      const auto e0 = __builtin_amdgcn_permlane16_swap(hr[8], hr[8], false, false), e1 = __builtin_amdgcn_permlane16_swap(hr[9], hr[9], false, false);
      st16((u32x4){e0[0], e1[0], e0[1], e1[1]}, rw, (g & 1) ? 0x80000000u : hb + 2048 + (g >> 1) * 256);
    }
    float s1 = 0.f, s2 = 0.f, s3 = 0.f, s4 = 0.f;
    const bf16x2 one2 = __builtin_bit_cast(bf16x2, 0x3F803F80u);
#pragma unroll
    for (int i = 0; i < 10; i += 2) {
      const bf16x2 u = __builtin_bit_cast(bf16x2, hr[i]), v = __builtin_bit_cast(bf16x2, hr[i + 1]);
      s1 = __builtin_amdgcn_fdot2_f32_bf16(u, one2, s1, false); s2 = __builtin_amdgcn_fdot2_f32_bf16(u, u, s2, false);
      s3 = __builtin_amdgcn_fdot2_f32_bf16(v, one2, s3, false); s4 = __builtin_amdgcn_fdot2_f32_bf16(v, v, s4, false);
    }
    s1 = sum4(s1 + s3); s2 = sum4(s2 + s4);
    if (lane < 16) st8((u32x2){__float_as_uint(s1), __float_as_uint(s2)}, rw, (unsigned)WS_ST + ct * 16384 + tok * 8);
    if (!YL) bar();
    publish(ws, tag, b * 192 + wid * 16, lane);
    if (YL) bar();
  }
  {
    const unsigned l16 = lane * 16, abase = WS_H + (rt1 * 8 + wid) * 20480, wb = opq(L0 + l16), wb2 = opq(L0 + 122880 + l16), cb_ = L0 + 121856 + g * 16;
    const unsigned hb = opq(WS_H1 + ((rt1 * 8 + wid) * 48 + ct1 * 3) * 1024 + l16);
    bf16x8 a[20], w[2][6];
    bar();
#pragma unroll
    for (int k = 0; k < DA1; ++k) a[k] = lda(rw, l16, abase, k);
    const unsigned so = (rt1 * 128 + wid * 16 + (lane & 15)) * 8 + g * 32768;
    float p0, p1;
    float nm;
    auto fin = [&](const f32x4& u, const f32x4& v, int m) {
      const f32x4 cu = *(const LAS f32x4*)(size_t)(cb_ + m * 128), cv = *(const LAS f32x4*)(size_t)(cb_ + m * 128 + 64);
      u32x4 o;
#pragma unroll
      for (int e = 0; e < 2; ++e) {
        unsigned t = cvtpk(__builtin_fmaf(nm, cu[2 * e], u[2 * e]), __builtin_fmaf(nm, cu[2 * e + 1], u[2 * e + 1])); asm("v_pk_max_i16 %0, %1, 0" : "=v"(o[e]) : "v"(t));
        t = cvtpk(__builtin_fmaf(nm, cv[2 * e], v[2 * e]), __builtin_fmaf(nm, cv[2 * e + 1], v[2 * e + 1])); asm("v_pk_max_i16 %0, %1, 0" : "=v"(o[2 + e]) : "v"(t));
      }
      st16(o, rw, hb + m * 1024);
    };
    f32x4 acc[6] = {};
    auto rdw = [&](int k, int c0, int c1) {
#pragma unroll
      for (int c = c0; c < c1; ++c) w[k & 1][c] = ldsr128(c < 4 ? wb + k * 4096 + c * 1024 : wb2 + k * 2048 + (c - 4) * 1024); };
    rdw(0, 0, KS > 0 ? 6 : 4);
    SB();
#pragma unroll
    for (int k = 0; k < 20; ++k) {
      if (k + 1 < 20) rdw(k + 1, 0, k + 1 < KS ? 6 : 4); else rdw(KS, 4, 6);
      if (k + DA1 < 20) a[k + DA1] = lda(rw, l16, abase, k + DA1);
      if (k + DA1 == 20) { p0 = __uint_as_float(__builtin_amdgcn_raw_buffer_load_b32(rw, so, WS_ST, SC1)); p1 = __uint_as_float(__builtin_amdgcn_raw_buffer_load_b32(rw, so, WS_ST + 16384, SC1)); }
#pragma unroll
      for (int c = 0; c < (k < KS ? 6 : 4); ++c) acc[c] = MFMA16(w[k & 1][c], a[k], acc[c]);
      ilv1<6>(); SB();
    }
    nm = sum4(p0 + p1) * (-1.f / D);
    q0 = __builtin_amdgcn_raw_buffer_load_b64(rw, tok * 8 + g * 32768, WS_ST, SC1); q1 = __builtin_amdgcn_raw_buffer_load_b64(rw, tok * 8 + g * 32768, WS_ST + 16384, SC1); SB();
    fin(acc[0], acc[1], 0); fin(acc[2], acc[3], 1);
    bar();
#pragma unroll
    for (int k = KS; k < 20; ++k) {
      if (k == PK) { asm volatile("" : "+v"(q0), "+v"(q1)); publish(ws, tag, b * 192 + wid * 16 + 2, lane); }
      if (k + 1 < 20) rdw(k + 1, 4, 6);
#pragma unroll
      for (int c = 4; c < 6; ++c) acc[c] = MFMA16(w[k & 1][c], a[k], acc[c]);
      ilv1<2>(); SB();
    }
    fin(acc[4], acc[5], 2);
    bar();
    if (wid < 4) { bar(); publish(ws, tag, b * 192 + wid * 16 + 1, lane); }
    if (wid < 4) __builtin_amdgcn_endpgm();
  }
  {
    const u32x2 p0 = q0, p1 = q1;
    const float s1 = sum4(__uint_as_float(p0[0]) + __uint_as_float(p1[0])), s2 = sum4(__uint_as_float(p0[1]) + __uint_as_float(p1[1]));
    const float mean = s1 * (1.f / D), ve = fmaxf(s2 * (1.f / D) - mean * mean, 0.f) + LN_EPS;
    float rs = __builtin_amdgcn_rsqf(ve);
    const float sd = __builtin_amdgcn_rcpf(rs);
    f32x4 acc2[2][5] = {};
#pragma unroll
    for (int c = 0; c < 5; ++c) acc2[0][c] = (f32x4){bflo(hr[2 * c]) * sd, bfhi(hr[2 * c]) * sd, bflo(hr[2 * c + 1]) * sd, bfhi(hr[2 * c + 1]) * sd};
    asm volatile("" : "+v"(acc2[0][0]), "+v"(acc2[0][1]), "+v"(acc2[0][2]), "+v"(acc2[0][3]), "+v"(acc2[0][4]), "+v"(rs));
    n80_loop<24, DA2>(rw, WS_H1 + (rt * 4 + rb) * 49152 + kh * 24576, WS_H1 + (rt * 4 + (rb ^ 1)) * 49152 + kh * 24576, opq(L0 + kh * 40960 + lane * 16), opq(L0 + 81920 + kh * 40960 + lane * 16), lane, acc2, ws, tag, b * 192 + wid * 16 + 1);
    n80_join(L0, rb, acc2);
    {
      f32x4 (&acc)[5] = acc2[0];
      unsigned o[10];
#pragma unroll
      for (int c = 0; c < 5; ++c) { o[2 * c] = cvtpk(acc[c][0] * rs, acc[c][1] * rs); o[2 * c + 1] = cvtpk(acc[c][2] * rs, acc[c][3] * rs); }
      char* op = out + (size_t)tok * 1280 + ct * 160 + g * 40;
      *(u32x4*)op = (u32x4){o[0], o[1], o[2], o[3]}; *(u32x4*)(op + 16) = (u32x4){o[4], o[5], o[6], o[7]}; *(u32x2*)(op + 32) = (u32x2){o[8], o[9]};
    }
  }
}

extern "C" void kernel_launch(void* const* d_in, const int* in_sizes, int n_in, void* d_out, int out_size, void* d_ws, size_t ws_size, hipStream_t stream) {
  const char* x = (const char*)d_in[0]; const char* wqkv = (const char*)d_in[1];
  const char* wo = (const char*)d_in[2]; const char* w1 = (const char*)d_in[4]; const char* w2 = (const char*)d_in[5];
  char* ws = (char*)d_ws;
  (void)hipFuncSetAttribute((const void*)mega_k, hipFuncAttributeMaxDynamicSharedMemorySize, 163840);
  mega_k<<<256, 768, 163840, stream>>>(x, wqkv, wo, w1, w2, ws, (char*)d_out);
}
```
